```python
import torch

def _softmax(x, axis=-1):
    return torch.softmax(x, dim=axis)

def _gelu(x):
    return torch.nn.functional.gelu(x, approximate='tanh')

def _silu(x):
    return torch.nn.functional.silu(x)

def _sigmoid(x):
    return torch.sigmoid(x)

def _layernorm(x, axis=-1, eps=1e-05):
    m = x.mean(dim=axis, keepdim=True)
    v = x.var(dim=axis, keepdim=True, unbiased=False)
    return (x - m) / torch.sqrt(v + eps)

def _scatter_add(tgt, idx, src, axis=0):
    return tgt.clone().index_add_(axis, idx, src)

def _segment_sum(src, seg, n):
    out = torch.zeros((n,) + src.shape[1:], dtype=src.dtype)
    return out.index_add_(0, seg, src)

def _pad(x, pw):
    flat = tuple((v for pair in reversed(pw) for v in pair))
    return torch.nn.functional.pad(x, flat)
kernel_name = 'standalone__tree_speculative_verify_tree_attention_over_candidate_token_tree__sampling'

def setup_inputs():

    _w = lambda a: (lambda t: t.to(torch.bfloat16) if t.is_floating_point() else t)(torch.as_tensor(a))
    B, Hq, Hkv, T, C, D = (16, 32, 8, 8, 2048, 128)
    tree_mask = torch.zeros((B, T, T), dtype=float)
    for b in range(B):
        parent = torch.zeros(T, dtype=torch.int64)
        for i in range(1, T):
            parent[i] = int(torch.randint(0, i, (1,)).item())
        for i in range(T):
            j = i
            tree_mask[b, i, j] = 1.0
            while j > 0:
                j = parent[j]
                tree_mask[b, i, j] = 1.0
    return {'q': _w((lambda *s: torch.randn(*s, dtype=torch.bfloat16) * 0.8)(B, Hq, T, D)), 'k_cache': _w((lambda *s: torch.randn(*s, dtype=torch.bfloat16) * 0.8)(B, Hkv, C, D)), 'v_cache': _w((lambda *s: torch.randn(*s, dtype=torch.bfloat16) * 0.05)(B, Hkv, C, D)), 'k_tree': _w((lambda *s: torch.randn(*s, dtype=torch.bfloat16) * 0.8)(B, Hkv, T, D)), 'v_tree': _w((lambda *s: torch.randn(*s, dtype=torch.bfloat16) * 0.05)(B, Hkv, T, D)), 'tree_mask': _w(tree_mask)}

def reference(q, k_cache, v_cache, k_tree, v_tree, tree_mask):
    B, Hq, T, D = q.shape
    Hkv, C = (k_cache.shape[1], k_cache.shape[2])
    rep = Hq // Hkv
    kc = (lambda x, n, axis: torch.repeat_interleave(x, n, dim=axis))(k_cache, rep, axis=1)
    vc = (lambda x, n, axis: torch.repeat_interleave(x, n, dim=axis))(v_cache, rep, axis=1)
    kt = (lambda x, n, axis: torch.repeat_interleave(x, n, dim=axis))(k_tree, rep, axis=1)
    vt = (lambda x, n, axis: torch.repeat_interleave(x, n, dim=axis))(v_tree, rep, axis=1)
    k = torch.concatenate([kc, kt], axis=2)
    v = torch.concatenate([vc, vt], axis=2)
    scale = D ** (-0.5)
    scores = torch.einsum('bhqd,bhkd->bhqk', q, k) * scale
    ctx_mask = torch.ones((B, 1, T, C), dtype=scores.dtype)
    tm = tree_mask[:, None, :, :].clone()
    full_mask = torch.concatenate([ctx_mask, tm], axis=-1)
    scores = scores + (1.0 - full_mask) * -1000000000.0
    p = _softmax(scores, axis=-1)
    return torch.einsum('bhqk,bhkd->bhqd', p, v)
```

```cpp
#include <hip/hip_runtime.h>

constexpr int C_ = 2048, NTILE = C_ / 32, NTAIL = 4;
constexpr int WIN = 8;
constexpr int PA = 10, PT = 440, PEND = 61;
constexpr float SL2E = 0.088388347648318440f * 1.4426950408889634f;

typedef unsigned u32x4 __attribute__((ext_vector_type(4)));
typedef unsigned u32x2 __attribute__((ext_vector_type(2)));
typedef float f32x2 __attribute__((ext_vector_type(2)));
typedef float f32x4 __attribute__((ext_vector_type(4)));
typedef float f32x16 __attribute__((ext_vector_type(16)));
typedef __bf16 bf16x8 __attribute__((ext_vector_type(8)));
typedef __bf16 bf16x2 __attribute__((ext_vector_type(2)));
typedef short s16x4 __attribute__((ext_vector_type(4)));
typedef __amdgpu_buffer_rsrc_t rsrc_t;
#define LAS __attribute__((address_space(3)))
#define DI __device__ __forceinline__
#define SB() __builtin_amdgcn_sched_barrier(0)
#pragma clang diagnostic ignored "-Wint-to-pointer-cast"

constexpr unsigned SLOT = 12288, RING = 12 * SLOT;
constexpr unsigned L_Q = RING, L_TK = L_Q + 8192, L_TV = L_TK + 2048, L_MASK = L_TV + 1024, L_ML = L_MASK + 256, L_FLAG = L_ML + 1024, L_END = L_TK + 8192;
constexpr unsigned F_FULL = L_FLAG, F_PROG = L_FLAG + 16;

struct Args { const char *q, *kc, *vc, *kt, *vt, *mask; char *out; };

DI f32x16 mfma32(u32x4 a, u32x4 b, f32x16 c) {
  return __builtin_amdgcn_mfma_f32_32x32x16_bf16(__builtin_bit_cast(bf16x8, a), __builtin_bit_cast(bf16x8, b), c, 0, 0, 0);
}
DI unsigned cvtpk(float lo, float hi) { f32x2 v = {lo, hi}; return __builtin_bit_cast(unsigned, __builtin_convertvector(v, bf16x2)); }
DI float hmax(float x) { auto t = __builtin_amdgcn_permlane32_swap(__float_as_uint(x), __float_as_uint(x), false, false); return fmaxf(__uint_as_float(t[0]), __uint_as_float(t[1])); }
DI float hsum(float x) { auto t = __builtin_amdgcn_permlane32_swap(__float_as_uint(x), __float_as_uint(x), false, false); return __uint_as_float(t[0]) + __uint_as_float(t[1]); }
DI float max3(float a, float b, float c) { return fmaxf(fmaxf(a, b), c); }
DI u32x4 lds128(unsigned a) { return *(const LAS u32x4*)a; }
DI u32x2 ldstr(unsigned a) { return __builtin_bit_cast(u32x2, __builtin_amdgcn_ds_read_tr16_b64_v4i16((LAS s16x4*)a)); }
DI rsrc_t mkrsrc(const void* p, unsigned bytes) { return __builtin_amdgcn_make_buffer_rsrc((void*)p, 0, bytes, 0x00020000); }
DI void flag_wr(unsigned a, int v) { *(volatile LAS int*)a = v; }

DI void spin_ge(unsigned a, int need) {
  int t;
  asm volatile(".Lspin%=:\n\tds_read_b32 %0, %1\n\ts_waitcnt lgkmcnt(0)\n\tv_cmp_le_i32 vcc, %2, %0\n\ts_cbranch_vccz .Lspin%=" : "=&v"(t) : "v"(a), "s"(need) : "vcc", "memory");
}
DI void spin_ge(unsigned a, int need, int t) {
  asm volatile(".Lspin%=:\n\tv_cmp_le_i32 vcc, %2, %0\n\ts_cbranch_vccnz .Ldone%=\n\tds_read_b32 %0, %1\n\ts_waitcnt lgkmcnt(0)\n\ts_branch .Lspin%=\n\t.Ldone%=:" : "+v"(t) : "v"(a), "s"(need) : "vcc", "memory");
}
DI void dma1(unsigned m0, unsigned v0, rsrc_t r, unsigned so) {
  asm volatile("s_nop 4\n\ts_mov_b32 m0, %0\n\ts_nop 0\n\tbuffer_load_dwordx4 %1, %2, %3 offen lds" :: "s"(m0), "v"(v0), "s"(r), "s"(so) : "memory");
}
template <int N> DI void vmw() { asm volatile("s_waitcnt vmcnt(%0)" :: "i"(N) : "memory"); }

__global__ __attribute__((aligned(4096))) __launch_bounds__(512) void tree_attn(Args A) {
  __shared__ __attribute__((aligned(1024))) char lds[L_END];
  const unsigned wg = blockIdx.x;
#define DMA4Q(a, b, c, d, rs, so) \
    "buffer_load_dwordx4 " a ", " rs ", " so " offen nt lds\n\t" \
    "buffer_load_dwordx4 " b ", " rs ", " so " offen offset:1024 nt lds\n\t" \
    "buffer_load_dwordx4 " c ", " rs ", " so " offen offset:2048 nt lds\n\t" \
    "buffer_load_dwordx4 " d ", " rs ", " so " offen offset:3072 nt lds\n\t"
#define DMA4(a, b, c, d, rs, so) "s_waitcnt vmcnt(%c[win])\n\tds_write_b32 v10, v11\n\tv_add_u32 v11, 1, v11\n\t" DMA4Q(a, b, c, d, rs, so)
  asm volatile(
    "v_readfirstlane_b32 s48, %[tid]\n\t"
    "v_and_b32 v13, 7, %[tid]\n\t"
    "v_mov_b32 v10, %[fl]\n\t"
    "v_lshl_or_b32 v14, v13, 2, v10\n\t"
    "s_cmp_gt_u32 s48, 63\n\t"
    "s_cbranch_scc0 .Lload%=\n\t"
    "v_mov_b32 v11, 0\n\t"
    "ds_write_b32 v14, v11\n\t"
    "s_waitcnt lgkmcnt(0)\n\t"
    "s_barrier\n\t"
    "s_branch .Lcons%=\n\t"
    ".Lload%=:\n\t"
    "s_load_dwordx2 s[56:57], %[ka], 0x0\n\t"
    "s_load_dwordx4 s[44:47], %[ka], 0x8\n\t"
    "s_setprio 3\n\t"
    "s_memrealtime s[34:35]\n\t"
    "s_and_b32 s48, %[wg], 7\n\t"
    "s_lshl_b32 s48, s48, 4\n\t"
    "s_lshr_b32 s49, %[wg], 4\n\t"
    "s_add_u32 s48, s48, s49\n\t"
    "s_lshl_b32 s53, s48, 13\n\t"
    "s_lshl_b32 s48, s48, 19\n\t"
    "s_lshl_b32 s49, %[wg], 4\n\t"
    "s_and_b32 s49, s49, 0x80\n\t"
    "v_lshrrev_b32 v12, 4, %[tid]\n\t"
    "v_and_b32 v11, 15, %[tid]\n\t"
    "v_xor_b32 v11, v11, v12\n\t"
    "v_lshlrev_b32 v11, 4, v11\n\t"
    "v_lshl_or_b32 v2, v12, 8, v11\n\t"
    "v_xor_b32 v3, 64, v2\n\t"
    "v_xor_b32 v4, 0x80, v2\n\t"
    "v_xor_b32 v5, 64, v4\n\t"
    "v_or_b32 v14, 16, v14\n\t"
    "v_lshrrev_b32 v12, 3, %[tid]\n\t"
    "v_lshlrev_b32 v13, 4, v13\n\t"
    "v_lshl_or_b32 v12, v12, 8, v13\n\t"
    "v_lshlrev_b32 v13, 2, %[tid]\n\t"
    "v_and_b32 v13, 64, v13\n\t"
    "v_xor_b32 v6, v12, v13\n\t"
    "v_add_u32 v7, 0x400, v6\n\t"
    "v_add_u32 v8, 0x800, v6\n\t"
    "v_add_u32 v9, 0xc00, v6\n\t"
    "v_mov_b32 v11, %c[n0]\n\t"
    "s_mov_b32 s38, 0x80000\n\t"
    "s_mov_b32 s39, 0x20000\n\t"
    "s_sub_u32 s42, s38, s49\n\t"
    "s_mov_b32 s43, s39\n\t"
    "s_add_u32 s49, s49, s48\n\t"
    "s_mov_b32 s50, 0\n\t"
    "s_mov_b32 s51, 0\n\t"
    "s_mov_b32 s52, %[lb]\n\t"
    "s_mov_b32 s58, 0x2000\n\t"
    "s_mov_b32 s59, s39\n\t"
    "s_mov_b32 m0, %[lq]\n\t"
    "s_movk_i32 s54, 0x1000\n\t"
    "s_barrier\n\t"
    "s_waitcnt lgkmcnt(0)\n\t"
    "s_add_u32 s56, s56, s53\n\t"
    "s_addc_u32 s57, s57, 0\n\t"
    DMA4Q("v2", "v3", "v4", "v5", "s[56:59]", "0")
    "s_add_u32 m0, m0, s54\n\t"
    "s_add_u32 s36, s44, s48\n\t"
    "s_addc_u32 s37, s45, 0\n\t"
    DMA4Q("v2", "v3", "v4", "v5", "s[56:59]", "s54")
    "s_lshl_b32 s34, s34, 4\n\t"
    "s_add_u32 s34, s34, %[pa]\n\t"
    "s_add_u32 s40, s46, s49\n\t"
    "s_addc_u32 s41, s47, 0\n\t"
    ".Ltile%=:\n\t"
    "s_mov_b32 m0, s52\n\t"
    DMA4("v2", "v3", "v4", "v5", "s[36:39]", "s51")
    "s_add_u32 m0, s52, 0x1000\n\t"
    "s_add_u32 s53, s51, 0x1000\n\t"
    DMA4("v2", "v3", "v4", "v5", "s[36:39]", "s53")
    "s_cmp_ge_u32 s50, %c[t0]\n\t"
    "s_cbranch_scc1 .Lnext%=\n\t"
    ".Lv%=:\n\t"
    "s_add_u32 m0, s52, 0x2000\n\t"
    DMA4("v6", "v7", "v8", "v9", "s[40:43]", "s51")
    ".Lnext%=:\n\t"
    "s_add_u32 s50, s50, 1\n\t"
    "s_add_u32 s51, s51, 0x2000\n\t"
    "s_add_u32 s52, s52, %[slot]\n\t"
    "s_cmp_eq_u32 s52, %[le]\n\t"
    "s_cselect_b32 s52, %[lb], s52\n\t"
    "s_cmp_lt_u32 s50, %[pend]\n\t"
    "s_cselect_b32 s53, %[pt], 0\n\t"
    "s_add_u32 s34, s34, s53\n\t"
    "s_cmp_lt_u32 s50, 12\n\t"
    "s_cbranch_scc1 .Ltile%=\n\t"
    "s_cmp_ge_u32 s50, 64\n\t"
    "s_cbranch_scc1 .Ltail%=\n\t"
    "s_and_b32 s53, s50, 3\n\t"
    "s_lshr_b32 s54, s50, 2\n\t"
    "s_sub_u32 s54, s54, 2\n\t"
    ".Lpoll%=:\n\t"
    "s_memrealtime s[30:31]\n\t"
    "ds_read_b32 v12, v14\n\t"
    "s_waitcnt lgkmcnt(0)\n\t"
    "v_readlane_b32 s55, v12, s53\n\t"
    "s_lshl_b32 s30, s30, 4\n\t"
    "s_sub_u32 s30, s30, s34\n\t"
    "s_cmp_ge_u32 s30, 0xffff8000\n\t"
    "s_cbranch_scc1 .Lpoll%=\n\t"
    "s_cmp_ge_i32 s55, s54\n\t"
    "s_cbranch_scc1 .Ltile%=\n\t"
    "s_branch .Lpoll%=\n\t"
    ".Ltail%=:\n\t"
    "s_cmp_eq_u32 s50, 64\n\t"
    "s_cselect_b32 s51, %[so0], s51\n\t"
    "s_cselect_b32 s52, %[sl0], s52\n\t"
    "s_cmp_lt_u32 s50, %[t1]\n\t"
    "s_cbranch_scc1 .Lv%=\n\t"
    ".set .Lk%=, %c[win]\n\t"
    ".rept %c[wg4]\n\t"
    "s_waitcnt vmcnt(.Lk%=)\n\tds_write_b32 v10, v11\n\tv_add_u32 v11, 1, v11\n\t"
    ".set .Lk%=, .Lk%= - 4\n\t"
    ".endr\n\t"
    "s_waitcnt lgkmcnt(0)\n\t"
    "s_endpgm\n\t"
    ".Lcons%=:"
    :: [tid] "v"(threadIdx.x), [ka] "s"(__builtin_amdgcn_kernarg_segment_ptr()), [wg] "s"(wg),
       [win] "i"(4 * WIN - 4), [wg4] "i"(WIN), [n0] "i"(2 - WIN), [pa] "i"(PA * 16), [pt] "i"(PT), [pend] "i"(PEND), [lq] "i"(L_Q), [fl] "i"(F_FULL), [lb] "i"(0), [le] "i"(RING), [slot] "i"(SLOT),
       [t0] "i"(NTILE - NTAIL), [t1] "i"(NTILE + NTAIL), [so0] "i"((NTILE - NTAIL) * 8192), [sl0] "i"(((NTILE - NTAIL) % 12) * SLOT)
    : "memory", "scc", "s30", "s31", "s34", "s35", "s36", "s37", "s38", "s39", "s40", "s41", "s42", "s43", "s44", "s45", "s46", "s47", "s48", "s49", "s50", "s51", "s52", "s53", "s54", "s55", "s56", "s57", "s58", "s59",
      "v2", "v3", "v4", "v5", "v6", "v7", "v8", "v9", "v10", "v11", "v12", "v13", "v14");
  const unsigned half = (wg >> 3) & 1, bh = (wg & 7) * 16 + (wg >> 4);
  const unsigned lane = threadIdx.x & 63, wv = __builtin_amdgcn_readfirstlane(threadIdx.x >> 6);
  const unsigned lb = (unsigned)(size_t)(LAS char*)lds;
  const unsigned r32 = lane & 31, h = lane >> 5, g4 = lane >> 4, c = lane & 15;
  const unsigned w = wv - 1;
  const bool tree = (w == 3);
  if (tree) {
    const rsrc_t tr = mkrsrc(A.kt + (size_t)bh * 2048, 2048);
    dma1(lb + L_TK, g4 * 256 + ((c ^ g4) << 4), tr, 0);
    dma1(lb + L_TK + 1024, g4 * 256 + ((c ^ (4 + g4)) << 4), tr, 1024);
    dma1(lb + L_TV, (lane >> 3) * 256 + (((lane & 7) ^ ((g4 & 1) << 2)) << 4), mkrsrc(A.vt + (size_t)bh * 2048 + half * 128, 2048 - half * 128), 0);
    const rsrc_t mr = mkrsrc(A.mask + (bh >> 3) * 128, 128);
    asm volatile("s_nop 4\n\ts_mov_b32 m0, %0\n\ts_nop 0\n\tbuffer_load_dword %1, %2, 0 offen lds" :: "s"(lb + L_MASK), "v"(lane * 4), "s"(mr) : "memory");
  }
  unsigned ka[8], va[2];
  ka[0] = lb + L_Q + r32 * 256 + ((h ^ c) << 4);
  asm volatile("" : "+v"(ka[0]));
#pragma unroll
  for (int s = 1; s < 8; ++s) ka[s] = ka[0] ^ (32 * s);
  {
    const unsigned q4 = c >> 2;
#pragma unroll
    for (int a = 0; a < 2; ++a)
      va[a] = (4 * h + q4) * 128 + (((((a ^ (w >> 1)) ^ (q4 >> 1)) << 2) | (2 * ((g4 ^ w) & 1) + ((lane & 3) >> 1))) << 4) + 8 * (lane & 1);
  }
  spin_ge(lb + F_FULL, 1);
  u32x4 qf[8];
#pragma unroll
  for (int s = 0; s < 8; ++s) qf[s] = lds128(ka[s]);

  float m = -INFINITY, l = 0.f;
  f32x16 o[2];
  {
    u32x4 z = {0u, 0u, 0u, 0u};
    asm volatile("" : "+v"(z));
    o[0] = mfma32(z, z, f32x16{});
    asm volatile("" : "+v"(z));
    o[1] = mfma32(z, z, f32x16{});
  }
  if (tree) {
    vmw<0>();
    f32x16 s16;
#pragma unroll
    for (int r = 0; r < 16; ++r) s16[r] = 0.f;
#pragma unroll
    for (int s = 0; s < 8; ++s) s16 = mfma32(lds128(ka[s] + (L_TK - L_Q)), qf[s], s16);
    const u32x2 mk = *(const LAS u32x2*)(lb + L_MASK + (r32 & 7) * 16 + h * 8);
    float x[4];
    constexpr float NEG = -1000000000.0f * 11.313708498984761f;
    x[0] = fmaf(1.f - __uint_as_float(mk[0] << 16), NEG, s16[0]);
    x[1] = fmaf(1.f - __uint_as_float(mk[0] & 0xffff0000u), NEG, s16[1]);
    x[2] = fmaf(1.f - __uint_as_float(mk[1] << 16), NEG, s16[2]);
    x[3] = fmaf(1.f - __uint_as_float(mk[1] & 0xffff0000u), NEG, s16[3]);
    m = hmax(fmaxf(fmaxf(x[0], x[1]), fmaxf(x[2], x[3])));
#pragma unroll
    for (int j = 0; j < 4; ++j) { x[j] = __builtin_amdgcn_exp2f((x[j] - m) * SL2E); l += x[j]; }
    const u32x4 p = {cvtpk(x[0], x[1]), cvtpk(x[2], x[3]), 0u, 0u};
#pragma unroll
    for (int a = 0; a < 2; ++a) { const u32x2 lo = ldstr(lb + L_TV + va[a]); o[a] = mfma32(u32x4{lo[0], lo[1], 0u, 0u}, p, o[a]); }
  }
  const unsigned cbase = lb + w * SLOT;
#pragma unroll
  for (int s = 0; s < 8; ++s) ka[s] += w * SLOT - L_Q;
#pragma unroll
  for (int a = 0; a < 2; ++a) va[a] += cbase + 8192;
  unsigned fa = lb + F_FULL, pa = lb + F_PROG + w * 4, mw = cbase + 4 * SLOT + lane * 16, mla = lb + L_ML + w * 256 + r32 * 8, mlx[3], mr[3];
#pragma unroll
  for (int x = 1; x < 4; ++x) { mlx[x - 1] = lb + L_ML + (w ^ x) * 256 + r32 * 8; mr[x - 1] = lb + ((w ^ x) + 4) * SLOT + (x - 1) * 2048 + lane * 16; }
  char* outp = A.out + ((size_t)bh * 32 + r32) * 256 + half * 128 + w * 32 + h * 16;
  asm volatile("" : "+v"(fa), "+v"(pa), "+v"(mw), "+v"(mla), "+v"(mlx[0]), "+v"(mlx[1]), "+v"(mlx[2]), "+v"(mr[0]), "+v"(mr[1]), "+v"(mr[2]), "+v"(outp));
  *(LAS f32x2*)mla = f32x2{-1.f, -1.f};
  int j3 = 0;
  float nms = m * -SL2E;
#pragma unroll 1
  for (int j = 0; j < NTILE / 4; ++j) {
    const bool lastr = NTAIL && j == NTILE / 4 - 1;
    const int gk = 12 * j + (lastr ? 2 : 3) * (int)w + 3, gv = lastr ? 12 * j + 10 + (int)w : gk + 1;
    const int step = (j3 == 2) ? -8 * (int)SLOT : 4 * (int)SLOT;
    j3 = (j3 == 2) ? 0 : j3 + 1;
    spin_ge(fa, gk);
    u32x4 kf[8];
#pragma unroll
    for (int s = 0; s < 8; ++s) kf[s] = lds128(ka[s]);
    SB();
    f32x16 s16;
#pragma unroll
    for (int r = 0; r < 16; ++r) s16[r] = 0.f;
#pragma unroll
    for (int s = 0; s < 8; ++s) { s16 = mfma32(kf[s], qf[s], s16); ka[s] += step; }
    float mx = max3(s16[0], s16[1], s16[2]), my = max3(s16[3], s16[4], s16[5]);
    mx = max3(mx, s16[6], s16[7]); my = max3(my, s16[8], s16[9]);
    mx = max3(mx, s16[10], s16[11]); my = max3(my, s16[12], s16[13]);
    mx = max3(mx, s16[14], s16[15]);
    mx = fmaxf(mx, my);
    auto sw = __builtin_amdgcn_permlane32_swap(__float_as_uint(mx), __float_as_uint(mx), false, false);
    const float mn = max3(__uint_as_float(sw[0]), __uint_as_float(sw[1]), m);
    float nm = mn * -SL2E;
    asm("" : "+v"(nm));
    const float alpha = __builtin_amdgcn_exp2f(nm - nms);
    m = mn; nms = nm;
#pragma unroll
    for (int r = 0; r < 16; ++r) s16[r] = __builtin_amdgcn_exp2f(fmaf(s16[r], SL2E, nm));
    u32x4 p[2];
#pragma unroll
    for (int s = 0; s < 2; ++s) p[s] = u32x4{cvtpk(s16[8 * s], s16[8 * s + 1]), cvtpk(s16[8 * s + 2], s16[8 * s + 3]), cvtpk(s16[8 * s + 4], s16[8 * s + 5]), cvtpk(s16[8 * s + 6], s16[8 * s + 7])};
    SB();
    const int seen = *(volatile LAS int*)fa;
    SB();
#pragma unroll
    for (int a = 0; a < 2; ++a) o[a] *= alpha;
    asm volatile("" : "+v"(p[0]), "+v"(p[1]), "+v"(o[0]), "+v"(o[1]));
    spin_ge(fa, gv, seen);
    u32x4 vf[2][2];
#pragma unroll
    for (int a = 1; a >= 0; --a)
#pragma unroll
      for (int s = 0; s < 2; ++s) {
        const u32x2 lo = ldstr(va[a] + s * 2048), hi = ldstr(va[a] + s * 2048 + 1024);
        vf[a][s] = u32x4{lo[0], lo[1], hi[0], hi[1]};
      }
    asm volatile("" ::: "memory");
    flag_wr(pa, j + 1);
    SB();
    f32x2 sum = {s16[0], s16[1]};
#pragma unroll
    for (int r = 2; r < 16; r += 2) sum += f32x2{s16[r], s16[r + 1]};
    l = fmaf(l, alpha, sum[0] + sum[1]);
    asm volatile("" : "+v"(l));
    SB();
#pragma unroll
    for (int a = 1; a >= 0; --a)
#pragma unroll
      for (int s = 0; s < 2; ++s) { o[a] = mfma32(vf[a][s], p[s], o[a]); SB(); }
#pragma unroll
    for (int a = 0; a < 2; ++a) va[a] += step;
  }
  *(LAS f32x2*)mla = f32x2{-nms, hsum(l)};
  SB();
#pragma unroll
  for (int x = 3; x > 0; --x)
#pragma unroll
    for (int g = 0; g < 2; ++g) { const int r0 = 8 * (x & 1) + 4 * g;
      *(LAS f32x4*)(mw + (x - 1) * 2048 + g * 1024) = f32x4{o[x >> 1][r0], o[x >> 1][r0 + 1], o[x >> 1][r0 + 2], o[x >> 1][r0 + 3]}; }
  f32x2 ml[4];
  asm volatile(".Lml%=:\n\tds_read_b64 v[40:41], %4\n\tds_read_b64 v[42:43], %5\n\tds_read_b64 v[44:45], %6\n\tds_read_b64 v[46:47], %7\n\ts_waitcnt lgkmcnt(0)\n\t"
               "v_cmp_eq_u32 vcc, -1.0, v43\n\tv_cmp_eq_u32 s[30:31], -1.0, v45\n\tv_cmp_eq_u32 s[34:35], -1.0, v47\n\ts_nop 0\n\t"
               "s_or_b64 vcc, vcc, s[30:31]\n\ts_or_b64 vcc, vcc, s[34:35]\n\ts_cbranch_scc1 .Lml%=\n\ts_barrier"
               : "={v[40:41]}"(ml[0]), "={v[42:43]}"(ml[1]), "={v[44:45]}"(ml[2]), "={v[46:47]}"(ml[3])
               : "{v48}"(mla), "{v49}"(mlx[0]), "{v50}"(mlx[1]), "{v51}"(mlx[2]) : "vcc", "scc", "s30", "s31", "s34", "s35", "memory");
  f32x4 v[3][2];
#pragma unroll
  for (int x = 1; x < 4; ++x)
#pragma unroll
    for (int g = 0; g < 2; ++g) v[x - 1][g] = *(const LAS f32x4*)(mr[x - 1] + g * 1024);
  SB();
  const float M = fmaxf(max3(ml[0][0], ml[1][0], ml[2][0]), ml[3][0]);
  float f[4], L = 0.f;
#pragma unroll
  for (int x = 0; x < 4; ++x) { f[x] = __builtin_amdgcn_exp2f(ml[x][0] - M); L = fmaf(f[x], ml[x][1], L); }
  const float inv = __builtin_amdgcn_rcpf(L);
  f32x4 y4[2];
#pragma unroll
  for (int g = 0; g < 2; ++g) y4[g] = (f32x4{o[0][4 * g], o[0][4 * g + 1], o[0][4 * g + 2], o[0][4 * g + 3]} * f[0] + v[0][g] * f[1] + v[1][g] * f[2] + v[2][g] * f[3]) * inv;
  const float y[8] = {y4[0][0], y4[0][1], y4[0][2], y4[0][3], y4[1][0], y4[1][1], y4[1][2], y4[1][3]};
  auto x0 = __builtin_amdgcn_permlane32_swap(cvtpk(y[0], y[1]), cvtpk(y[4], y[5]), false, false);
  auto x1 = __builtin_amdgcn_permlane32_swap(cvtpk(y[2], y[3]), cvtpk(y[6], y[7]), false, false);
  *(__attribute__((address_space(1))) u32x4*)outp = u32x4{x0[0], x1[0], x0[1], x1[1]};
}

extern "C" void kernel_launch(void* const* d_in, const int* in_sizes, int n_in, void* d_out, int out_size, void* d_ws, size_t ws_size, hipStream_t stream) {
  Args a{(const char*)d_in[0], (const char*)d_in[1], (const char*)d_in[2], (const char*)d_in[3], (const char*)d_in[4], (const char*)d_in[5], (char*)d_out};
  tree_attn<<<256, 320, 0, stream>>>(a);
}
```
